```python
import torch

def _softmax(x, axis=-1):
    return torch.softmax(x, dim=axis)
def _gelu(x):
    return torch.nn.functional.gelu(x, approximate="tanh")
def _silu(x):
    return torch.nn.functional.silu(x)
def _sigmoid(x):
    return torch.sigmoid(x)
def _layernorm(x, axis=-1, eps=1e-5):
    m = x.mean(dim=axis, keepdim=True)
    v = x.var(dim=axis, keepdim=True, unbiased=False)
    return (x - m) / torch.sqrt(v + eps)
def _scatter_add(tgt, idx, src, axis=0):
    return tgt.clone().index_add_(axis, idx, src)
def _segment_sum(src, seg, n):
    out = torch.zeros((n,) + src.shape[1:], dtype=src.dtype)
    return out.index_add_(0, seg, src)
def _pad(x, pw):
    flat = tuple(v for pair in reversed(pw) for v in pair)
    return torch.nn.functional.pad(x, flat)

kernel_name = 'gated_mix__softmax_full_mha__mixer_s_mlp__prompt'

def setup_inputs():

    _w = (lambda a: (lambda t: t.to(torch.bfloat16) if t.is_floating_point() else t)(torch.as_tensor(a)))
    def _A_setup():
        B, S, D = 1, 2048, 512
        return {
            "x": _w((lambda *s: torch.randn(*s, dtype=torch.bfloat16) * 0.05)(B, S, D)),
            "wqkv": _w((lambda *s: torch.randn(*s, dtype=torch.bfloat16) * 1.6)(D, 3 * D)),
            "wo": _w((lambda *s: torch.randn(*s, dtype=torch.bfloat16) * 0.1)(D, D)),
        }
    _A_in = _A_setup()

    def _B_setup():
        B, S, D, F = 1, 2048, 512, 1792
        return {
            "x":  _w((lambda *s: torch.randn(*s, dtype=torch.bfloat16) * 0.05)(B, S, D)),
            "w1": _w((lambda *s: torch.randn(*s, dtype=torch.bfloat16) * 3.2)(D, D) * (D ** -0.5)),
            "w2": _w((lambda *s: torch.randn(*s, dtype=torch.bfloat16) * 3.2)(D, D) * (D ** -0.5)),
            "w3": _w((lambda *s: torch.randn(*s, dtype=torch.bfloat16) * 3.2)(D, F) * (D ** -0.5)),
            "w4": _w((lambda *s: torch.randn(*s, dtype=torch.bfloat16) * 1.6)(F, D) * (F ** -0.5)),
        }
    _B_in = _B_setup()

    return {
        "A_x": _A_in["x"],
        "A_wqkv": _A_in["wqkv"],
        "A_wo": _A_in["wo"],
        "B_x": _B_in["x"],
        "B_w1": _B_in["w1"],
        "B_w2": _B_in["w2"],
        "B_w3": _B_in["w3"],
        "B_w4": _B_in["w4"],
        "gate_w": _w((lambda *s: torch.randn(*s, dtype=torch.bfloat16) * 0.05)(512) * 16.0),
    }

def reference(A_x, A_wqkv, A_wo, B_x, B_w1, B_w2, B_w3, B_w4, gate_w):
    def _A_ref(x, wqkv, wo):
        B, S, D = x.shape
        H = 32; Dh = D // H
        qkv = (lambda x, *d: x.permute(*d))((x @ wqkv).reshape(B, S, 3, H, Dh), 2, 0, 3, 1, 4)
        q, k, v = qkv[0], qkv[1], qkv[2]
        scale = Dh ** -0.5
        scores = torch.einsum("bhqd,bhkd->bhqk", q, k) * scale
        p = _softmax(scores, axis=-1)
        o = torch.einsum("bhqk,bhkd->bhqd", p, v)
        return (lambda x, *d: x.permute(*d))(o, 0, 2, 1, 3).reshape(B, S, D) @ wo

    def _B_ref(x, w1, w2, w3, w4):
        B, S, D = x.shape
        h = _gelu(_layernorm(x, axis=-1) @ w1)
        c = D // 4
        g0 = _pad(h[:, :, 0*c:1*c], ((0, 0), (1, 0), (0, 0)))[:, :S, :]
        g1 = _pad(h[:, :, 1*c:2*c], ((0, 0), (0, 1), (0, 0)))[:, 1:, :]
        g2 = _pad(h[:, :, 2*c:3*c], ((0, 0), (2, 0), (0, 0)))[:, :S, :]
        g3 = _pad(h[:, :, 3*c:   ], ((0, 0), (0, 2), (0, 0)))[:, 2:, :]
        hs = torch.concatenate([g0, g1, g2, g3], axis=-1) @ w2
        x1 = x + hs
        h2 = _gelu(_layernorm(x1, axis=-1) @ w3) @ w4
        return x1 + h2

    x = A_x
    g = _sigmoid((x * gate_w).sum(-1, keepdims=True))
    a = _A_ref(x, A_wqkv, A_wo)
    b = _B_ref(x, B_w1, B_w2, B_w3, B_w4)
    return g * a + (1 - g) * b
```

```cpp
#include <hip/hip_runtime.h>
#include <hip/hip_bf16.h>

typedef unsigned short u16;
typedef unsigned int u32;
typedef unsigned long long u64;
typedef __bf16 bf16x2 __attribute__((ext_vector_type(2)));
typedef __bf16 bf16x8 __attribute__((ext_vector_type(8)));
typedef __bf16 bf16x16 __attribute__((ext_vector_type(16)));
typedef short s16x4 __attribute__((ext_vector_type(4)));
typedef float f32x2 __attribute__((ext_vector_type(2)));
typedef float f32x4 __attribute__((ext_vector_type(4)));
typedef float f32x8 __attribute__((ext_vector_type(8)));
typedef float f32x16 __attribute__((ext_vector_type(16)));
typedef u32 u32x2 __attribute__((ext_vector_type(2)));
typedef u32 u32x4 __attribute__((ext_vector_type(4)));
#define LAS __attribute__((address_space(3)))
#define GAS __attribute__((address_space(1)))
#define DI __device__ __forceinline__
typedef LAS char* lptr;

constexpr int GAP_G3 = 6, GAP_G4 = 6;
constexpr u32 HOLD_A = 610, HOLD_B = 660;
constexpr int FSTR = 128;

constexpr int S = 2048, D = 512, F = 1792;
constexpr int HROWS = S + 4;
constexpr u32 WS_Q = 0, WS_K = WS_Q + (2u << 20), WS_V = WS_K + (2u << 20), WS_H = WS_V + (2u << 20);
constexpr u32 WS_X1 = WS_H + 64 * HROWS * 16, WS_HID = WS_X1 + (2u << 20), WS_O = WS_HID + S * F * 2;
constexpr u32 WS_RST = WS_O + (2u << 20), WS_CS = WS_RST + S * 16, WS_ST = WS_CS + 16384, WS_FL = WS_ST + 16 * S * 8, WS_END = WS_FL + 2112 * FSTR;
constexpr int FL_X = 0, FL_1 = 16, FL_S = FL_1 + 256, FL_C = FL_S + 128, FL_2 = FL_C + 144, FL_3 = FL_2 + 256, FL_4 = FL_3 + 256, FL_O = FL_4 + 256, FL_START = FL_O + 256;

#define MFMA(a, b, c) __builtin_amdgcn_mfma_f32_32x32x16_bf16((a), (b), (c), 0, 0, 0)
#define WAITV(n) asm volatile("s_waitcnt vmcnt(%0)" ::"n"(n) : "memory")
#define BAR() asm volatile("s_barrier" ::: "memory")
#define LBAR() asm volatile("s_waitcnt lgkmcnt(0)\n\ts_barrier" ::: "memory")
#define SB() __builtin_amdgcn_sched_barrier(0)
#define SG(mask, n) __builtin_amdgcn_sched_group_barrier(mask, n, 0)
#define PRIO(n) __builtin_amdgcn_s_setprio(n)
#define GAP(n) do { if ((n) >= 0) asm volatile("s_nop %0" ::"n"((n) < 0 ? 0 : (n))); SB(); } while (0)

DI void dma16c(u32 voff, const void* sbase, u32 ldsdst) {
    asm volatile("s_mov_b32 m0, %2\n\ts_nop 0\n\tglobal_load_lds_dwordx4 %0, %1 sc1" ::"v"(voff), "s"(sbase), "s"(ldsdst) : "memory");
}
#define DMA4(SC) asm volatile("s_mov_b32 m0, %5\n\ts_nop 0\n\tglobal_load_lds_dwordx4 %0, %4" SC "\n\tglobal_load_lds_dwordx4 %1, %4 offset:1024" SC \
    "\n\tglobal_load_lds_dwordx4 %2, %4 offset:2048" SC "\n\tglobal_load_lds_dwordx4 %3, %4 offset:3072" SC ::"v"(v[0]), "v"(v[1]), "v"(v[2]), "v"(v[3]), "s"(sbase), "s"(ldsdst) : "memory")
DI void dma4(u32x4 v, const void* sbase, u32 ldsdst) { DMA4(""); }
DI void dma4c(u32x4 v, const void* sbase, u32 ldsdst) { DMA4(" sc1"); }
DI void dma2(u32x4 v, const void* sbase, u32 ldsdst) {
    asm volatile("s_mov_b32 m0, %3\n\ts_nop 0\n\tglobal_load_lds_dwordx4 %0, %2\n\tglobal_load_lds_dwordx4 %1, %2 offset:1024" ::"v"(v[0]), "v"(v[1]), "s"(sbase), "s"(ldsdst) : "memory");
}
DI u32x4 voff4(u32 v, u32 stride) { return u32x4{v, v + (stride - 1024), v + 2 * (stride - 1024), v + 3 * (stride - 1024)}; }
DI void touch(const void* p, u32 ldsdst) { asm volatile("s_mov_b32 m0, %1\n\ts_nop 0\n\tglobal_load_lds_dword %0, off" ::"v"(p), "s"(ldsdst) : "memory"); }
DI void fadd(float& a, float b) { asm("v_add_f32 %0, %0, %1" : "+v"(a) : "v"(b)); }
DI void fmac(float& a, float b, float c) { asm("v_fmac_f32 %0, %1, %2" : "+v"(a) : "v"(b), "v"(c)); }
DI bf16x8 ldb128(lptr p) { return *(LAS const bf16x8*)p; }
DI bf16x8 ldtr(lptr p0, lptr p1) {
    s16x4 a = __builtin_amdgcn_ds_read_tr16_b64_v4i16((LAS s16x4*)p0);
    s16x4 b = __builtin_amdgcn_ds_read_tr16_b64_v4i16((LAS s16x4*)p1);
    return __builtin_bit_cast(bf16x8, __builtin_shufflevector(a, b, 0, 1, 2, 3, 4, 5, 6, 7));
}
DI u32 pk2(float lo, float hi) { bf16x2 v = {(__bf16)lo, (__bf16)hi}; return __builtin_bit_cast(u32, v); }
DI float blo(u32 w) { return __uint_as_float(w << 16); }
DI float bhi(u32 w) { return __uint_as_float(w & 0xffff0000u); }
DI f32x2 unp(u32 w) { return f32x2{blo(w), bhi(w)}; }
DI float xhalf(float v) {
    auto r = __builtin_amdgcn_permlane32_swap(__float_as_uint(v), __float_as_uint(v), false, false);
    return __uint_as_float((threadIdx.x & 32) ? r[0] : r[1]);
}
#define WSR(ws) __builtin_amdgcn_make_buffer_rsrc(ws, 0, WS_END, 0x00020000)
DI void st16(char* ws, u32 off, u32x4 v) { __builtin_amdgcn_raw_buffer_store_b128(v, WSR(ws), off, 0, 16); }
DI void st8(char* ws, u32 off, u32x2 v) { __builtin_amdgcn_raw_buffer_store_b64(v, WSR(ws), off, 0, 16); }
DI void st4(char* ws, u32 off, u32 v) { __builtin_amdgcn_raw_buffer_store_b32(v, WSR(ws), off, 0, 16); }
DI u32x4 ld16(char* ws, u32 off) { return __builtin_amdgcn_raw_buffer_load_b128(WSR(ws), off, 0, 16); }
DI f32x2 ld8(char* ws, u32 off) { return __builtin_bit_cast(f32x2, __builtin_amdgcn_raw_buffer_load_b64(WSR(ws), off, 0, 16)); }
DI float ld4(char* ws, u32 off) { return __uint_as_float(__builtin_amdgcn_raw_buffer_load_b32(WSR(ws), off, 0, 16)); }
DI u64 ldflag(char* ws, int idx) { return __hip_atomic_load((GAS u64*)(ws + WS_FL + idx * FSTR), __ATOMIC_RELAXED, __HIP_MEMORY_SCOPE_AGENT); }
DI void stflag(char* ws, int idx, u64 v) { __hip_atomic_store((GAS u64*)(ws + WS_FL + idx * FSTR), v, __ATOMIC_RELAXED, __HIP_MEMORY_SCOPE_AGENT); }
template <int NAP = 1>
DI void poll(char* ws, int idx, u64 tag) {
    bool ok = false;
#pragma unroll 1
    for (u32 spins = 0; spins < (1u << 18); ++spins) {
        if (!ok) ok = ldflag(ws, idx) == tag;
        if (__all(ok)) break;
        __builtin_amdgcn_s_sleep(NAP);
    }
    asm volatile("" ::: "memory");
}
DI void publish(char* ws, int w, int lane, int idx, u64 tag) {
    WAITV(0); LBAR();
    if (w == 7 && lane == 0) stflag(ws, idx, tag);
}

constexpr int RS = 16384, LDS_BYTES = 5 * RS;
template <int NST, int NSL, int P, class IA, class IW>
DI void ring_tail(IA ia, IW iw, int s0) {
    int slot = (s0 - 1) % NSL;
#pragma unroll 1
    for (int s = s0; s <= NST - NSL; ++s) { WAITV((NSL - 2) * P); BAR(); ia(s + NSL - 1, slot); iw(s + NSL - 1, slot); slot = slot == NSL - 1 ? 0 : slot + 1; }
#pragma unroll
    for (int k = NSL - 2; k >= 0; --k) { WAITV(k * P); BAR(); }
}
template <int NST, class IA, class IW>
DI void ring_load(IA ia, IW iw) {
    ia(0, 0); iw(0, 0); ia(1, 1); iw(1, 1);
    WAITV(8); BAR(); ia(2, 2); iw(2, 2); ia(3, 3); iw(3, 3);
    WAITV(16); BAR(); ia(4, 4); iw(4, 4); ia(5, 0); iw(5, 0);
    ring_tail<NST, 5, 8>(ia, iw, 2);
}
template <int NST, int NSL, int PW, class IA, class IW>
DI void ring_load_pre(IA ia, IW iw) {
#pragma unroll
    for (int t = 0; t < NSL; ++t) iw(t, t);
    BAR();
    ia(0, 0); ia(1, 1);
    WAITV(4); BAR(); ia(2, 2); ia(3, 3);
    WAITV(8); BAR();
#pragma unroll
    for (int t = 4; t < NSL; ++t) ia(t, t);
    ia(NSL, 0); iw(NSL, 0);
#pragma unroll
    for (int k = 2; k < NSL - 1; ++k) { WAITV(4 * (NSL - 1 - k) + (k - 1) * (4 + PW)); BAR(); ia(NSL + k - 1, k - 1); iw(NSL + k - 1, k - 1); }
    ring_tail<NST, NSL, 4 + PW>(ia, iw, NSL - 1);
}

template <bool VT>
DI void mm128(lptr lds, int w, int lane, f32x16 (&acc)[2][2]) {
    const int r = lane & 31, h = lane >> 5, q = (lane & 15) >> 2, p = lane & 3, blk = (lane >> 4) & 1, wm = w & 1, wn = w >> 1;
    const int arow = wm * 64 + (VT ? 16 * ((r >> 2) & 1) + (r & 3) + 4 * (r >> 3) : r);
    const u32 a0 = arow * 64 + ((h ^ ((arow >> 2) & 3)) << 4), a1 = a0 ^ 32;
    constexpr u32 MI = 2048;
    const int chl = VT ? 2 * blk + (p >> 1) : 2 * (p & 1) + blk, sub = VT ? 8 * (p & 1) : 8 * (p >> 1);
    const u32 wb0 = 8192 + (8 * h + q) * 256 + (((8 * wn + chl) ^ (q << 2)) << 4) + sub, wb1 = wb0 ^ 64;
    bf16x8 a[3][2], f[3][2];
    auto rd = [&](int g) {
        lptr sl = lds + ((g >> 1) % 5) * RS; const int ks = g & 1, e = g % 3;
        a[e][0] = ldb128(sl + (ks ? a1 : a0)); f[e][0] = ldtr(sl + wb0 + ks * 4096, sl + wb0 + ks * 4096 + 1024);
        f[e][1] = ldtr(sl + wb1 + ks * 4096, sl + wb1 + ks * 4096 + 1024); a[e][1] = ldb128(sl + (ks ? a1 : a0) + MI);
    };
    BAR();
    rd(0); rd(1);
#pragma unroll
    for (int g = 0; g < 32; ++g) {
        if (!(g & 1) && g < 30) LBAR();
        SB();
        if (g < 30) rd(g + 2);
#pragma unroll
        for (int mi = 0; mi < 2; ++mi)
#pragma unroll
            for (int ni = 0; ni < 2; ++ni) {
                const f32x16 c = g ? acc[mi][ni] : f32x16{};
                acc[mi][ni] = VT ? MFMA(a[g % 3][mi], f[g % 3][ni], c) : MFMA(f[g % 3][ni], a[g % 3][mi], c);
            }
        asm volatile("" : "+v"(acc[0][0]), "+v"(acc[0][1]), "+v"(acc[1][0]), "+v"(acc[1][1]));
        SG(0x8, 1); SG(0x100, 3); SG(0x8, 1); SG(0x100, 3); SG(0x8, 2); SB();
    }
}
DI auto w128_issue(u32 ldsb, int lw, int lane, const char* wb, u32 pitch, int rot) {
    const u32x4 wv = voff4((lane >> 4) * pitch + (((lane & 15) ^ ((lane >> 4) << 2)) << 4), 4 * pitch);
    wb += (size_t)(16 * lw) * pitch;
    return [=](int t, int slot) { dma4(wv, wb + (u32)((t ^ rot) * 32) * pitch, ldsb + slot * RS + 8192 + lw * 4096); };
}
DI void x_load(u32 ldsb, int lw, int lane, const char* x, int m0, const char* wb, u32 pitch, int rot) {
    const u32x4 av = voff4((lane >> 2) * 1024 + (((lane & 3) ^ (lane >> 4)) << 4), 16384);
    const char* xb = x + (size_t)(m0 + 64 * lw) * 1024;
    ring_load<16>([=](int t, int slot) { dma4(av, xb + (t ^ rot) * 64, ldsb + slot * RS + lw * 4096); }, w128_issue(ldsb, lw, lane, wb, pitch, rot));
}
DI void ln_gelu_tile(const f32x16& acc, const f32x4 (&c)[4], f32x2 sv, char* ws, u32 a, u32 kbs) {
    const float rs = sv[1], nm = -sv[0], ca = -2.302208198f * rs, cb = ca * (0.044715f * rs * rs), ir = __builtin_amdgcn_rcpf(rs);
    f32x2 v[8], t[8];
#pragma unroll
    for (int i = 0; i < 8; ++i) v[i] = f32x2{c[i >> 1][(i & 1) * 2], c[i >> 1][(i & 1) * 2 + 1]} * nm + f32x2{acc[2 * i], acc[2 * i + 1]};
#pragma unroll
    for (int i = 0; i < 8; ++i) t[i] = v[i] * v[i];
#pragma unroll
    for (int i = 0; i < 8; ++i) t[i] = t[i] * cb + ca;
#pragma unroll
    for (int i = 0; i < 8; ++i) t[i] = t[i] * v[i];
    SB();
#pragma unroll
    for (int i = 0; i < 8; ++i) t[i] = f32x2{__builtin_amdgcn_exp2f(t[i][0]), __builtin_amdgcn_exp2f(t[i][1])};
    SB();
#pragma unroll
    for (int i = 0; i < 8; ++i) t[i] = t[i] * ir + ir;
    SB();
#pragma unroll
    for (int i = 0; i < 8; ++i) t[i] = f32x2{__builtin_amdgcn_rcpf(t[i][0]), __builtin_amdgcn_rcpf(t[i][1])};
    SB();
    u32 o[8];
#pragma unroll
    for (int i = 0; i < 8; ++i) v[i] = v[i] * t[i];
#pragma unroll
    for (int i = 0; i < 8; ++i) o[i] = pk2(v[i][0], v[i][1]);
    st16(ws, a, u32x4{o[0], o[1], o[2], o[3]}); st16(ws, a + kbs, u32x4{o[4], o[5], o[6], o[7]});
    SB();
}
DI void ld_cs(f32x4 (&c)[4], lptr p) {
#pragma unroll
    for (int i = 0; i < 4; ++i) c[i] = *(LAS f32x4*)(p + i * 16);
}
constexpr int E_CS = RS, E_ST = RS + 512;

constexpr int R3 = 12288, G_CS = 6 * R3, G_ST = G_CS + 512;
DI void g3_compute(lptr lds, int w, int lane, char* ws, u32 off) {
    const int r = lane & 31, h = lane >> 5, q = (lane & 15) >> 2, p = lane & 3, blk = (lane >> 4) & 1, wm = w & 1, wn = w >> 1;
    const u32 ab = h * 2048 + (wm * 64 + r) * 16;
    const u32 wb = 8192 + (8 * h + q) * 128 + (((4 * wn + 2 * (p & 1) + blk) ^ (((q >> 1) & 1) << 2)) << 4) + 8 * (p >> 1);
    off += (wn * 4 + h * 2) * (S * 16) + (wm * 64 + r) * 16;
    f32x16 acc[2];
    bf16x8 a[2][2][2], f[2][2];
    auto rd = [&](int s, int ks) {
        lptr sl = lds + (s % 6) * R3;
        a[s & 1][ks][0] = ldb128(sl + ab + ks * 4096); f[s & 1][ks] = ldtr(sl + wb + ks * 2048, sl + wb + ks * 2048 + 512); a[s & 1][ks][1] = ldb128(sl + ab + ks * 4096 + 512);
    };
    auto mm = [&](int s, int ks, int i) { acc[i] = MFMA(f[s & 1][ks], a[s & 1][ks][i], ((s & 15) | ks) ? acc[i] : f32x16{}); asm volatile("" : "+v"(acc[i])); SB(); };
    BAR();
    rd(0, 0); rd(0, 1);
#pragma unroll
    for (int ps = 0; ps < 2; ++ps) {
#pragma unroll
        for (int u = 0; u < 16; ++u) {
            const int s = ps * 16 + u;
            if (s == 19) WAITV(0);
            if (s < 31) LBAR();
            mm(s, 0, 0); if (s < 31) rd(s + 1, 0); GAP(GAP_G3 - 1);
            mm(s, 0, 1); if (s < 31) rd(s + 1, 1); GAP(GAP_G3 - 1);
            mm(s, 1, 0); GAP(GAP_G3 + 3);
            mm(s, 1, 1); GAP(GAP_G3 + 1);
        }
        PRIO(3);
        f32x4 c[4];
        ld_cs(c, lds + G_CS + (ps * 64 + wn * 32 + h * 16) * 4);
        const f32x2 s0 = *(LAS f32x2*)(lds + G_ST + (wm * 64 + r) * 8), s1 = *(LAS f32x2*)(lds + G_ST + (wm * 64 + 32 + r) * 8);
        if (ps) LBAR();
        ln_gelu_tile(acc[0], c, s0, ws, off + ps * 8 * (S * 16), S * 16);
        ln_gelu_tile(acc[1], c, s1, ws, off + ps * 8 * (S * 16) + 512, S * 16);
    }
}

template <int MODE>
DI void g64_load(u32 ldsb, int lw, int lane, const char* ab, const char* wb, const char* wb2, int g) {
    constexpr u32 KBS = MODE == 0 ? HROWS * 16 : S * 16;
    asm volatile("" : "+s"(ldsb), "+s"(lw), "+s"(g));
    ab += (size_t)(4 * lw) * KBS; wb += lw * 32768; wb2 += lw * 32768;
    const u32x4 av = voff4(lane * 16, KBS), wv = voff4((lane >> 3) * 1024 + (((lane & 7) ^ (((lane >> 4) & 1) << 2)) << 4), 8192);
    auto k64 = [=](int t) {
        if (MODE == 0) return t ^ (4 * g);
        int u = (t < 14 ? 2 * t : 2 * t - 27) + 14 * g;
        u = u >= 28 ? u - 28 : u;
        return __builtin_amdgcn_readfirstlane(t < 28 ? u : 28 + ((t - 28) ^ (4 * g)));
    };
    auto ia = [=](int t, int slot) {
        const int q = k64(t) >> 1, sh = MODE == 0 ? ((q & 1) ? 16 : -16) * (1 + (q >> 1)) : 0;
        dma4c(av, ab + (u32)k64(t) * (8 * KBS) + sh, ldsb + slot * RS + lw * 4096);
    };
    auto iw = [=](int t, int slot) { dma4(wv, (t < 28 ? wb : wb2) + (u32)k64(t) * 65536u, ldsb + slot * RS + 8192 + lw * 4096); };
    BAR();
    ring_load<MODE == 1 ? 36 : 8>(ia, iw);
}
template <int NST, int SP>
DI f32x16 mm64(lptr lds, int w, int lane, f32x16& y) {
    const int r = lane & 31, h = lane >> 5, q = (lane & 15) >> 2, p = lane & 3, blk = (lane >> 4) & 1, mt2 = w & 1, nt2 = w >> 1;
    const u32 abase = h * 1024 + (mt2 * 32 + r) * 16;
    const u32 wbase = 8192 + (8 * h + q) * 128 + (((4 * nt2 + 2 * (p & 1) + blk) ^ (((q >> 1) & 1) << 2)) << 4) + 8 * (p >> 1);
    f32x16 acc = {};
    bf16x8 a[2][4], f[2][4];
    auto rd = [&](int slot, int set, int ks) { lptr sl = lds + slot * RS; a[set][ks] = ldb128(sl + abase + ks * 2048); f[set][ks] = ldtr(sl + wbase + ks * 2048, sl + wbase + ks * 2048 + 512); };
    auto mm = [&](int set, int k, bool first = false) { acc = MFMA(f[set][k], a[set][k], first ? f32x16{} : acc); asm volatile("" : "+v"(acc)); SB(); };
    auto step = [&](int u, bool drain, bool first = false) {
        if (drain) WAITV(0);
        LBAR();
        mm(u & 1, 0, first); rd((u + 1) % 5, (u + 1) & 1, 0); rd((u + 1) % 5, (u + 1) & 1, 1); GAP(SP - 3);
        mm(u & 1, 1); rd((u + 1) % 5, (u + 1) & 1, 2); rd((u + 1) % 5, (u + 1) & 1, 3); GAP(SP - 3);
        mm(u & 1, 2); GAP(SP + 3);
        mm(u & 1, 3); GAP(SP + 1);
    };
    asm volatile("" : "+v"(acc));
    BAR();
    rd(0, 0, 0); rd(0, 0, 1); rd(0, 0, 2); rd(0, 0, 3);
    if (NST == 36) {
#pragma unroll 1
        for (int s = 0; s < 2; ++s) {
#pragma unroll
            for (int u = 0; u < 10; ++u) step(u, u == 2);
        }
#pragma unroll
        for (int u = 0; u < 8; ++u) step(u, false);
        y = acc;
#pragma unroll
        for (int u = 8; u < 15; ++u) step(u, false, u == 8);
    } else {
#pragma unroll
        for (int u = 0; u < 7; ++u) step(u, false);
    }
    mm(1, 0); mm(1, 1); mm(1, 2); mm(1, 3);
    return acc;
}
DI void bars(int n) {
#pragma unroll 1
    for (int i = 0; i < n; ++i) BAR();
}

template <int NL>
DI void colsum_job(lptr lds, int w, int lane, const char* wb, u32 pitch, u32 dst, char* ws) {
    const char* p = wb + (size_t)(8 * NL * w + (lane >> 3)) * pitch + (lane & 7) * 16;
    u32x4 v[NL];
#pragma unroll
    for (int k = 0; k < NL; ++k) v[k] = *(const u32x4*)(p + (size_t)(8 * k) * pitch);
    float c[8] = {};
#pragma unroll
    for (int k = 0; k < NL; ++k)
#pragma unroll
        for (int i = 0; i < 4; ++i) { fadd(c[2 * i], blo(v[k][i])); fadd(c[2 * i + 1], bhi(v[k][i])); }
#pragma unroll
    for (int m = 8; m < 64; m <<= 1)
#pragma unroll
        for (int i = 0; i < 8; ++i) fadd(c[i], __shfl_xor(c[i], m));
    if (lane < 8) { *(LAS f32x4*)(lds + w * 256 + lane * 32) = f32x4{c[0], c[1], c[2], c[3]}; *(LAS f32x4*)(lds + w * 256 + lane * 32 + 16) = f32x4{c[4], c[5], c[6], c[7]}; }
    LBAR();
    if (w == 0) {
        float t = 0.f;
#pragma unroll
        for (int i = 0; i < 8; ++i) t += *(LAS float*)(lds + i * 256 + lane * 4);
        st4(ws, dst + lane * 4, __float_as_uint(t));
    }
}
struct Args { const u16 *x, *wqkv, *wo, *w1, *w2, *w3, *w4, *gw; char* ws; u16* out; };
DI void side_jobs(const Args& A, lptr lds, int w, int lane, int i, u64 tag, const char* code, u32 code_bytes) {
    char* ws = A.ws;
    const u32 t0 = (u32)__builtin_amdgcn_s_memrealtime();
    PRIO(3);
    if (w == 5 && (u32)((i >> 3) * 8192 + lane * 128 + 4) <= code_bytes) touch(code + (i >> 3) * 8192 + lane * 128, (u32)(size_t)lds + 4608);
    if (w == 4 && lane < 9) { const u32 o = WS_FL + (i * 9 + lane) * 128; touch(ws + (o < WS_END ? o : WS_FL), (u32)(size_t)lds + 4864); }
    auto ahead = [&]() {
        constexpr int K0 = 64, NW = (512 - K0) * 32, PER = (NW + 4096) / 512;
        if (w >= 6 && lane < PER) {
            const int li = ((w - 6) * 256 + i) * PER + lane, cb = (li & 31) * 128, kr = K0 + (li >> 5);
            touch(li >= NW ? (const char*)A.w2 + (li - NW) * 128
                           : cb >= 3072 ? (const char*)A.w1 + kr * 1024 + (cb - 3072) : (const char*)A.wqkv + kr * 3072 + cb, (u32)(size_t)lds + 4096 + (w - 6) * 256);
        }
    };
    if (i >= 128 && i < 160) {
        colsum_job<2>(lds, w, lane, (const char*)A.w1 + ((i - 128) >> 2) * 128 + (i & 3) * 128 * 1024, 1024, WS_CS + ((i & 3) * 512 + ((i - 128) >> 2) * 64) * 4, ws);
        publish(ws, w, lane, FL_C + i - 128, tag);
        ahead();
    } else {
        if (w >= 6) {
            if (w == 6) __builtin_amdgcn_s_sleep(4); else __builtin_amdgcn_s_sleep(12);
            ahead();
        } else if (i < 128 && w < 4) {
            const int row = 16 * i + 4 * w + (lane >> 4);
            const u32x4* xp = (const u32x4*)(A.x + row * D) + (lane & 15); const u32x4* gp = (const u32x4*)A.gw + (lane & 15);
            u32x4 xv[4], gv[4];
#pragma unroll
            for (int k = 0; k < 4; ++k) { xv[k] = xp[16 * k]; gv[k] = gp[16 * k]; }
            float a = 0.f, b = 0.f, c = 0.f, a2 = 0.f, b2 = 0.f, c2 = 0.f;
#pragma unroll
            for (int e = 0; e < 16; ++e) {
                const u32 xx = xv[e >> 2][e & 3], gg = gv[e >> 2][e & 3];
                fadd(a, blo(xx)); fmac(b, blo(xx), blo(xx)); fmac(c, blo(xx), blo(gg)); fadd(a2, bhi(xx)); fmac(b2, bhi(xx), bhi(xx)); fmac(c2, bhi(xx), bhi(gg));
            }
            fadd(a, a2); fadd(b, b2); fadd(c, c2);
#pragma unroll
            for (int m = 1; m < 16; m <<= 1) { fadd(a, __shfl_xor(a, m)); fadd(b, __shfl_xor(b, m)); fadd(c, __shfl_xor(c, m)); }
            const float mean = a * (1.0f / D), rs = __builtin_amdgcn_rsqf(__builtin_fmaxf(b * (1.0f / D) - mean * mean, 0.f) + 1e-5f);
            const float g = __builtin_amdgcn_rcpf(1.0f + __builtin_amdgcn_exp2f(-1.4426950408889634f * c));
            if ((lane & 15) == 0) st16(ws, WS_RST + row * 16, u32x4{__float_as_uint(mean), __float_as_uint(rs), __float_as_uint(g), 0u});
            WAITV(0);
        }
        if (i < 128) {
            BAR();
            if (w == 7 && lane == 0) stflag(ws, FL_S + i, tag);
        }
    }
    if (w == 7) { const u32 hold = i < 192 ? HOLD_B : HOLD_A; while ((u32)__builtin_amdgcn_s_memrealtime() - t0 < hold) __builtin_amdgcn_s_sleep(4); }
    WAITV(0); BAR();
}

constexpr int VT = 1120, ASLOT = 8192 + 8 * VT, A_Q = 4 * ASLOT, A_FLAG = A_Q + 8192;
DI f32x16 smf(bf16x8 v, const f32x16& pp, int half, f32x16 o) {
    f32x8 b = half ? __builtin_shufflevector(pp, pp, 8, 9, 10, 11, 12, 13, 14, 15) : __builtin_shufflevector(pp, pp, 0, 1, 2, 3, 4, 5, 6, 7);
    return __builtin_amdgcn_smfmac_f32_32x32x32_bf16(v, __builtin_bit_cast(bf16x16, b), o, 0xdddd, 0, 0);
}
DI float rowmax(const f32x16& s, float m) {
#pragma unroll
    for (int i = 0; i < 16; i += 2) m = __builtin_fmaxf(__builtin_fmaxf(m, s[i]), s[i + 1]);
    return m;
}
#define A_E(r) "v_exp_f32 v" #r ", v" #r "\n\t"
#define A_TILE(c0,c1,c2,c3,c4,c5,c6,c7,c8,c9,c10,c11,c12,c13,c14,c15, n0,n8,n15, RV1,RK,RV0, V1A,V1O, KA,KO, V0A,V0O) \
    A_E(c0) A_E(c1) A_E(c2) "v_smfmac_f32_32x32x32_bf16 v[0:15], " RV1 ", v[" #n8 ":" #n15 "], %[idx]\n\t" \
    "s_waitcnt lgkmcnt(2)\n\tds_read_b128 " RV1 ", " #V1A " offset:" #V1O "\n\t" \
    A_E(c3) A_E(c4) A_E(c5) "v_mfma_f32_32x32x16_bf16 v[" #n0 ":" #n15 "], " RK ", v[64:67], v[16:31]\n\t" \
    "ds_read_b128 " RK ", " #KA " offset:" #KO "\n\t" \
    A_E(c6) A_E(c7) A_E(c8) A_E(c9) A_E(c10) "v_smfmac_f32_32x32x32_bf16 v[0:15], " RV0 ", v[" #c0 ":" #c7 "], %[idx]\n\t" \
    "ds_read_b128 " RV0 ", " #V0A " offset:" #V0O "\n\t" \
    A_E(c11) A_E(c12) A_E(c13) A_E(c14) A_E(c15)
#define A_EVEN(V1A,V1O,KA,KO,V0A,V0O) A_TILE(32,33,34,35,36,37,38,39,40,41,42,43,44,45,46,47, 48,56,63, "v[88:91]", "v[80:83]", "v[68:71]", V1A,V1O,KA,KO,V0A,V0O)
#define A_ODD(V1A,V1O,KA,KO,V0A,V0O) A_TILE(48,49,50,51,52,53,54,55,56,57,58,59,60,61,62,63, 32,40,47, "v[76:79]", "v[72:75]", "v[84:87]", V1A,V1O,KA,KO,V0A,V0O)
#define A_REGS "+{v[0:15]}"(o), "+{v[32:47]}"(sa), "+{v[48:63]}"(sb), "+{v[68:71]}"(v0a), "+{v[72:75]}"(ke), "+{v[76:79]}"(v1a), "+{v[80:83]}"(ko), "+{v[84:87]}"(v0b), "+{v[88:91]}"(v1b)
#define A_HEAD1 A_EVEN(v94, 1120, v92, 3072, v93, 2240) A_ODD(v94, 2240, v92, 4096, v93, 3360) A_EVEN(v94, 3360, v92, 5120, v93, 4480)
#define A_HEAD2 A_ODD(v94, 4480, v92, 6144, v93, 5600) A_EVEN(v94, 5600, v92, 7168, v93, 6720)
#define A_DMA(base, dst) "s_cbranch_vccz 3f\n\ts_setprio 3\n\ts_add_u32 m0, %[t], " dst "\n\ts_nop 0\n\tglobal_load_lds_dwordx4 %[voff], " base " sc1\n\t"
#define A_LOOP(HI, LO) \
    "1:\n\ts_cmp_lt_u32 %[c], 7\n\ts_cbranch_scc1 2f\n\ts_setprio " LO "\n\ts_branch 4f\n2:\n\ts_setprio " HI "\n4:\n\t" \
    "s_add_u32 %[t], %[c], 1\n\ts_and_b32 %[t], %[t], 3\n\ts_mul_i32 %[t], %[t], %[aslot]\n\tv_mov_b32 v95, v93\n\tv_mov_b32 v96, v94\n\t" \
    "v_add_u32 v92, %[t], %[ka]\n\tv_add_u32 v93, %[t], %[va0]\n\tv_add_u32 v94, %[t], %[va1]\n\t" \
    "s_sub_u32 %[t], %[c], 1\n\ts_cmp_lt_u32 %[t], 4\n\ts_cselect_b64 vcc, -1, 0\n\ts_and_b32 %[t], %[t], 3\n\ts_mul_i32 %[t], %[t], %[aslot]\n\t" \
    A_ODD(v96, 6720, v92, 0, v95, 7840) \
    A_DMA("%[kp]", "%[kd]") "s_setprio " HI "\n3:\n\t" \
    A_EVEN(v96, 7840, v92, 1024, v93, 0) \
    A_DMA("%[vp]", "%[vd]") "s_setprio " HI "\n3:\n\t" \
    A_ODD(v94, 0, v92, 2048, v93, 1120) \
    "s_cmp_eq_u32 %[c], 7\n\ts_cbranch_scc1 9f\n\tv_add_u32 %[voff], 0x2000, %[voff]\n\t" \
    A_HEAD1 \
    "s_add_u32 %[c], %[c], 1\n\ts_cmp_eq_u32 %[c], 7\n\ts_cbranch_scc1 5f\n\ts_setprio " LO "\n5:\n\t" \
    A_HEAD2 \
    "s_cmp_lt_u32 %[c], 6\n\ts_cbranch_scc1 6f\n\ts_cmp_eq_u32 %[c], 7\n\ts_cbranch_scc1 1b\n\ts_waitcnt vmcnt(0)\n6:\n\t" \
    "s_waitcnt vmcnt(2)\n\ts_barrier\n\ts_branch 1b\n9:\n\ts_waitcnt lgkmcnt(0)\n\ts_nop 1"
DI void attention(lptr lds, u32 ldsb, int w, int lane, int head, int qt, char* ws, u64 tag) {
    const int r = lane & 31, h = lane >> 5;
    const char* kg = ws + WS_K + head * 65536;
    const char* vg = ws + WS_V + head * 65536;
    const u32 voff = lane * 16 + w * 1024;
    auto issue = [&](int c) {
        const u32 d = ldsb + (c & 3) * ASLOT;
        dma16c(voff, kg + c * 8192, d + w * 1024); dma16c(voff, vg + c * 8192, d + 8192 + w * VT);
    };
    if (lane < 16) { const u32 c = (lane & 1) ? 0u : 0x3f803f80u; *(LAS u32x4*)(lds + (lane >> 2) * ASLOT + 8192 + w * VT + 1024 + (lane & 1) * 16 + (lane & 2) * 32) = u32x4{c, c, c, c}; }
    if (w == 0 && lane == 0) *(LAS u32*)(lds + A_FLAG) = 0u;
    const int fkv = FL_1 + (4 + ((lane >> 4) & 1) * 4 + (head >> 3)) * 16;
    if (w == 7) poll(ws, lane < 32 ? fkv + (lane & 3) : FL_1 + (head >> 3) * 16 + 2 * qt + (lane & 1), tag);
    BAR();
    dma16c(voff, ws + WS_Q + (head * 64 + qt * 8) * 1024, ldsb + A_Q + w * 1024);
    const u32 cofs = ((r >= 20 && r < 28) ? 1088 : 1024) + ((r & 27) == 16 ? 0 : 16);
    const u32 va0 = 8192 + (r < 16 ? h * 256 + r * 16 : cofs), va1 = 8192 + (r < 16 ? 512 + h * 256 + r * 16 : cofs), ka = lane * 16;
    f32x16 o; bf16x8 qf;
#pragma unroll 1
    for (int attempt = 0; attempt < 2; ++attempt) {
        issue(0);
        float m = -3.0e38f;
        if (attempt) {
#pragma unroll 1
            for (int t = 0; t < 64; ++t) m = rowmax(MFMA(__builtin_bit_cast(bf16x8, ld16(ws, WS_K + head * 65536 + t * 1024 + lane * 16)), qf, f32x16{}), m);
        }
        WAITV(0); LBAR();
        issue(1);
        u64 fl = 0;
        if (w == 7) fl = ldflag(ws, fkv + (lane & 15));
        qf = ldb128(lds + A_Q + voff);
        bf16x8 ke = ldb128(lds + ka);
        if (!attempt) m = rowmax(MFMA(ke, qf, f32x16{}), m);
        m = __builtin_fmaxf(m, xhalf(m));
        f32x16 ci;
#pragma unroll
        for (int i = 0; i < 16; ++i) ci[i] = -m;
        f32x16 sa = MFMA(ke, qf, ci), sb = {};
        bf16x8 ko = ldb128(lds + ka + 1024); ke = ldb128(lds + ka + 2048);
        bf16x8 v0a = ldb128(lds + va0), v0b = ldb128(lds + va0 + VT);
        bf16x8 v1a = ldb128(lds + va1), v1b = v1a;
        o = f32x16{};
        asm volatile("s_waitcnt lgkmcnt(0)\n\ts_nop 15" : A_REGS : : "memory");
        u32 c92 = ldsb + ka, c93 = ldsb + va0, c94 = ldsb + va1, c95, c96;
        asm volatile(A_HEAD1 "s_cmp_lt_u32 %[w], 4\n\ts_cbranch_scc1 7f\n\ts_setprio 0\n7:\n\t" A_HEAD2
                     : A_REGS : "{v[16:31]}"(ci), "{v[64:67]}"(qf), [idx] "v"(0xdddd), "{v92}"(c92), "{v93}"(c93), "{v94}"(c94), [w] "s"(w) : "memory", "scc");
        if (w == 7 && !__all(fl == tag)) poll(ws, fkv + (lane & 15), tag);
        asm volatile("s_waitcnt vmcnt(0)\n\ts_barrier" ::"v"(fl) : "memory");
        PRIO(3); issue(2); issue(3);
        {
            u32 c = 0, t, vo = voff;
            const char *kp = kg + 3 * 8192, *vp = vg + 3 * 8192;
#define A_OPS : A_REGS, "+{v92}"(c92), "+{v93}"(c93), "+{v94}"(c94), "=&{v95}"(c95), "=&{v96}"(c96), [c] "+s"(c), [t] "=&s"(t), [voff] "+v"(vo) \
              : "{v[16:31]}"(ci), "{v[64:67]}"(qf), [idx] "v"(0xdddd), [ka] "v"(ldsb + ka), [va0] "v"(ldsb + va0), [va1] "v"(ldsb + va1), [aslot] "s"(ASLOT), \
                [w] "s"(w), [kp] "s"(kp), [vp] "s"(vp), [kd] "s"(ldsb + w * 1024), [vd] "s"(ldsb + 8192 + w * VT) : "memory", "vcc", "scc"
            asm volatile("s_cmp_lt_u32 %[w], 4\n\ts_cbranch_scc0 8f\n\t" A_LOOP("1", "1") "\n\ts_branch 7f\n8:\n\t" A_LOOP("2", "0") "\n7:\n.Lmix_end:" A_OPS);
        }
        o = smf(v1b, sb, 1, o);
        float big = o[8];
#pragma unroll
        for (int i = 0; i < 8; ++i) big = __builtin_fmaxf(big, __builtin_fabsf(o[i]) * 5.4e-20f);
        bool bad = !(big < 1.8e19f);
#pragma unroll
        for (int i = 0; i < 9; ++i) bad |= (o[i] != o[i]);
        if (__any(bad) && lane == 0) *(LAS u32*)(lds + A_FLAG) = 1u;
        const float il = __builtin_amdgcn_rcpf(o[8]);
        st16(ws, WS_O + ((head * 2 + h) * S + (qt * 8 + w) * 32 + r) * 16,
             u32x4{pk2(o[0] * il, o[1] * il), pk2(o[2] * il, o[3] * il), pk2(o[4] * il, o[5] * il), pk2(o[6] * il, o[7] * il)});
        WAITV(0); LBAR();
        if (attempt || *(LAS volatile u32*)(lds + A_FLAG) == 0u) break;
        LBAR();
    }
    if (w == 7 && lane == 0) stflag(ws, FL_O + qt * 32 + head, tag);
}

__global__ __launch_bounds__(512, 4) void mix_kernel(const Args A) {
    extern __shared__ __attribute__((aligned(16))) char smem[];
    lptr lds = (lptr)smem; const u32 ldsb = (u32)(size_t)lds;
    char* ws = A.ws;
    const int lane = threadIdx.x & 63, w = __builtin_amdgcn_readfirstlane(threadIdx.x >> 6);
    const int b = blockIdx.x, i = b & 255, xcd = b & 7, j = i >> 3;
    const int r = lane & 31, h = lane >> 5;
    const char* code; u32 code_bytes;
    asm volatile("s_getpc_b64 %0\n.Lmix_pc:\n\ts_mov_b32 %1, .Lmix_end-.Lmix_pc" : "=s"(code), "=s"(code_bytes));
    u64 tag = 0;
    if (w >= 6) {
        const u64 t = ldflag(ws, FL_X) + 1;
        tag = ((u64)(u32)__builtin_amdgcn_readfirstlane((u32)(t >> 32)) << 32) | (u32)__builtin_amdgcn_readfirstlane((u32)t);
        if (w == 7 && lane == 0) stflag(ws, FL_START + b, tag);
    }
    if (w >= 4) PRIO(3);
    const int mt1 = 4 * (xcd & 3) + (j & 3), nt1 = (j >> 3) * 4 + (xcd >> 2) * 2 + ((j >> 2) & 1);
    if ((b < 256) == (nt1 < 12)) {
        const bool hh = nt1 >= 12;
        if (w >= 6) {
            if (hh && w == 7) {
                const int fi = lane < 8 ? FL_S + mt1 * 8 + lane : FL_C + (nt1 - 12) * 8 + (lane & 7);
                bars(11);
                const u64 fl = ldflag(ws, fi);
                bars(3);
                if (!__all(fl == tag)) poll(ws, fi, tag);
                float c[2][4]; f32x2 sv[2];
#pragma unroll
                for (int u = 0; u < 2; ++u) {
#pragma unroll
                    for (int k = 0; k < 4; ++k) c[u][k] = ld4(ws, WS_CS + (k * 512 + (nt1 - 12) * 128 + u * 64 + lane) * 4);
                    sv[u] = ld8(ws, WS_RST + (mt1 * 128 + u * 64 + lane) * 16);
                }
                if ((mt1 == 0 || mt1 == 15) && lane < 32)
                    st16(ws, WS_H + (((nt1 - 12) * 16 + (lane >> 1)) * HROWS + (mt1 == 0 ? 0 : S + 2) + (lane & 1)) * 16, u32x4{0, 0, 0, 0});
                bars(2);
#pragma unroll
                for (int u = 0; u < 2; ++u) {
                    asm volatile("" : "+v"(c[u][0]), "+v"(c[u][1]), "+v"(c[u][2]), "+v"(c[u][3]));
                    *(LAS float*)(lds + E_CS + (u * 64 + lane) * 4) = (c[u][0] + c[u][1]) + (c[u][2] + c[u][3]); *(LAS f32x2*)(lds + E_ST + (u * 64 + lane) * 8) = sv[u];
                }
                LBAR();
            } else { bars(16); if (hh) BAR(); }
        } else if (w >= 4) {
            x_load(ldsb, w - 4, lane, (const char*)A.x, mt1 * 128, hh ? (const char*)A.w1 + (nt1 - 12) * 256 : (const char*)A.wqkv + nt1 * 256, hh ? 1024u : 3072u, ((mt1 ^ nt1) & 1) * 8);
            if (hh) BAR();
        } else {
            f32x16 acc[2][2];
            const int wm = w & 1, wn = w >> 1;
            if ((nt1 >> 2) == 2) {
                mm128<true>(lds, w, lane, acc);
                const int d = r & 15, ds = (d & 3) | ((d & 4) << 1) | ((d & 8) >> 1);
#pragma unroll
                for (int mi = 0; mi < 2; ++mi)
#pragma unroll
                    for (int ni = 0; ni < 2; ++ni) {
                        const f32x16& a = acc[mi][ni];
                        const u32 o = WS_V + (((nt1 & 3) * 8 + wn * 4 + ni * 2 + (r >> 4)) * 64 + mt1 * 4 + wm * 2 + mi) * 1024 + h * 512 + ds * 16;
                        st16(ws, o, u32x4{pk2(a[0], a[1]), pk2(a[2], a[3]), pk2(a[4], a[5]), pk2(a[6], a[7])});
                        st16(ws, o + 256, u32x4{pk2(a[8], a[9]), pk2(a[10], a[11]), pk2(a[12], a[13]), pk2(a[14], a[15])});
                    }
            } else {
                mm128<false>(lds, w, lane, acc);
                if (hh) {
                    PRIO(3);
                    LBAR();
                    const u32 o = WS_H + (((nt1 - 12) * 16 + wn * 8 + h * 2) * HROWS + mt1 * 128 + 2 + wm * 64 + r) * 16;
                    f32x4 c0[4], c1[4];
                    ld_cs(c0, lds + E_CS + (wn * 64 + h * 16) * 4);
                    const f32x2 s0 = *(LAS f32x2*)(lds + E_ST + (wm * 64 + r) * 8), s1 = *(LAS f32x2*)(lds + E_ST + (wm * 64 + 32 + r) * 8);
                    ln_gelu_tile(acc[0][0], c0, s0, ws, o, HROWS * 16);
                    ld_cs(c1, lds + E_CS + (wn * 64 + 32 + h * 16) * 4);
                    ln_gelu_tile(acc[1][0], c0, s1, ws, o + 512, HROWS * 16);
                    ln_gelu_tile(acc[0][1], c1, s0, ws, o + 4 * HROWS * 16, HROWS * 16);
                    ln_gelu_tile(acc[1][1], c1, s1, ws, o + 4 * HROWS * 16 + 512, HROWS * 16);
                } else {
                    const float sc = nt1 < 4 ? 0.25f * 1.4426950408889634f : 1.0f;
#pragma unroll
                    for (int mi = 0; mi < 2; ++mi)
#pragma unroll
                        for (int ni = 0; ni < 2; ++ni) {
                            const f32x16 a = acc[mi][ni] * sc;
                            const u32 o = (nt1 < 4 ? WS_Q : WS_K) + ((((nt1 & 3) * 8 + wn * 4 + ni * 2 + h) * 64 + mt1 * 4 + wm * 2 + mi) * 64 + r) * 16;
                            st16(ws, o, u32x4{pk2(a[0], a[1]), pk2(a[2], a[3]), pk2(a[4], a[5]), pk2(a[6], a[7])});
                            st16(ws, o + 512, u32x4{pk2(a[8], a[9]), pk2(a[10], a[11]), pk2(a[12], a[13]), pk2(a[14], a[15])});
                        }
                }
            }
        }
        publish(ws, w, lane, FL_1 + nt1 * 16 + mt1, tag);
    } else side_jobs(A, lds, w, lane, i, tag, code, code_bytes);
    if (b < 256) {
        if (w >= 4) PRIO(2); else PRIO(1);
        attention(lds, ldsb, w, lane, 4 * xcd + (j >> 3), j & 7, ws, tag);
        return;
    }
    const int mt = 4 * xcd + (j & 3), nt = j >> 2, m3 = 2 * xcd + (j & 1), n3 = j >> 1;
    const int m = mt * 64 + (w & 1) * 32 + r, nc = nt * 64 + (w >> 1) * 32 + h * 16;
    const int rot = (mt ^ nt) & 1;
    const u32 x1o = WS_X1 + ((nc >> 3) * S + m) * 16;
    if (w >= 6) {
        if (w == 7) { const int t0 = (mt * 64 - 2 < 0 ? 0 : mt * 64 - 2) >> 7, t1 = (mt * 64 + 65 > S - 1 ? S - 1 : mt * 64 + 65) >> 7; poll<12>(ws, FL_1 + (12 + (lane & 3)) * 16 + ((lane & 4) ? t1 : t0), tag); }
        bars(9);
    } else if (w >= 4) g64_load<0>(ldsb, w - 4, lane, ws + WS_H + (mt * 64 + 2) * 16, (const char*)A.w2 + nt * 128, nullptr, rot);
    else {
        const u32x4 e0 = *(const u32x4*)(A.x + m * D + nc), e1 = *(const u32x4*)(A.x + m * D + nc + 8);
        BAR();
        f32x16 none;
        const f32x16 acc = mm64<8, -9>(lds, w, lane, none);
        PRIO(3);
        u32 o[8]; f32x2 s1 = {0.f, 0.f}, s2 = {0.f, 0.f};
#pragma unroll
        for (int k = 0; k < 8; ++k) {
            const f32x2 y = f32x2{acc[2 * k], acc[2 * k + 1]} + unp(k < 4 ? e0[k & 3] : e1[k & 3]);
            o[k] = pk2(y[0], y[1]); const f32x2 u = unp(o[k]); s1 += u; s2 += u * u;
        }
        st16(ws, x1o, u32x4{o[0], o[1], o[2], o[3]}); st16(ws, x1o + S * 16, u32x4{o[4], o[5], o[6], o[7]});
        float a1 = s1[0] + s1[1], a2 = s2[0] + s2[1]; a1 += xhalf(a1); a2 += xhalf(a2);
        if (h == 0) st8(ws, WS_ST + ((nt * 2 + (w >> 1)) * S + m) * 8, u32x2{__float_as_uint(a1), __float_as_uint(a2)});
    }
    publish(ws, w, lane, FL_2 + (mt >> 1) * 16 + (mt & 1) * 8 + nt, tag);
    if (j >= 28) {
        const int k0 = xcd * 4 + j - 28;
        if (k0 < 28) {
            colsum_job<8>(lds, w, lane, (const char*)A.w3 + k0 * 128, F * 2, WS_CS + (2048 + k0 * 64) * 4, ws);
            publish(ws, w, lane, FL_C + 32 + k0, tag);
        }
        const int li = (k0 * 8 + w) * 72 + lane, l2 = li + 64;
        touch(li < 14336 ? (const char*)A.w4 + li * 128 : (const char*)A.wo + (li - 14336) * 128, ldsb + w * 256);
        if (lane < 8) touch(l2 < 14336 ? (const char*)A.w4 + l2 * 128 : (const char*)A.wo + (l2 - 14336) * 128, ldsb + w * 256);
        WAITV(0);
        if (b == 511 && w == 7) {
#pragma unroll 1
            for (int k = 0; k < 8; ++k) poll(ws, FL_START + k * 64 + lane, tag);
            if (lane == 0) stflag(ws, FL_X, tag);
        }
    }
    if (w >= 6) {
        const int f3 = (mt >> 1) * 16 + (lane < 14 ? lane : 0);
        if (j < 28) {
            if (w == 7) poll(ws, FL_2 + m3 * 16 + (lane & 15), tag);
            BAR();
            const int n = (w - 6) * 64 + lane, fi = FL_C + 32 + n3 * 2 + (lane & 1);
            f32x2 p[16];
#pragma unroll
            for (int k = 0; k < 16; ++k) p[k] = ld8(ws, WS_ST + (k * S + m3 * 128 + n) * 8);
            bars(6);
            u64 fl = ldflag(ws, fi);
            float t0 = 0.f, t1 = 0.f;
#pragma unroll
            for (int k = 0; k < 16; ++k) { asm volatile("" : "+v"(p[k])); fadd(t0, p[k][0]); fadd(t1, p[k][1]); }
            const float mean = t0 * (1.0f / D);
            *(LAS f32x2*)(lds + G_ST + n * 8) = f32x2{mean, __builtin_amdgcn_rsqf(__builtin_fmaxf(t1 * (1.0f / D) - mean * mean, 0.f) + 1e-5f)};
            bars(4);
            if (!__all(fl == tag)) poll(ws, fi, tag);
            const float c = ld4(ws, WS_CS + (2048 + n3 * 128 + n) * 4);
            bars(4);
            *(LAS float*)(lds + G_CS + n * 4) = c;
            LBAR(); bars(6);
            if (w == 7 && lane == 0) stflag(ws, FL_3 + m3 * 16 + n3, tag);
            bars(6);
            fl = ldflag(ws, FL_3 + f3);
            bars(5);
            if (w == 7 && !__all(fl == tag)) poll(ws, FL_3 + f3, tag);
        } else if (w == 7) poll<4>(ws, FL_3 + f3, tag);
        BAR();
        bars(4);
        if (j < 28 && w == 7 && lane == 0) stflag(ws, FL_4 + m3 * 16 + n3, tag);
        bars(3);
        const u64 fl = ldflag(ws, FL_4 + f3);
        bars(3);
        if (w == 7 && !__all(fl == tag)) poll(ws, FL_4 + f3, tag);
        bars(11);
        const u64 fo = ldflag(ws, FL_O + (mt >> 2) * 32 + (lane & 31));
        bars(3);
        if (w == 7 && !__all(fo == tag)) poll(ws, FL_O + (mt >> 2) * 32 + (lane & 31), tag);
        bars(12);
    } else if (w >= 4) {
        const int lw = w - 4;
        if (j < 28) {
            const char* ab = ws + WS_X1 + 2 * lw * (S * 16) + m3 * 128 * 16;
            const char* wb = (const char*)A.w3 + n3 * 256 + 16 * lw * (F * 2);
            const u32x4 av = {(u32)lane * 16, (u32)lane * 16, lane * 16 + S * 16 - 2048u, lane * 16 + S * 16 - 2048u};
            const u32x4 wv = voff4((lane >> 3) * (F * 2) + (((lane & 7) ^ (((lane >> 4) & 1) << 2)) << 4), 8 * F * 2);
            ring_load_pre<32, 6, 2>([=](int t, int slot) { dma4c(av, ab + (u32)(t & 15) * (4 * S * 16), ldsb + slot * R3 + lw * 4096); },
                                    [=](int t, int slot) { dma2(wv, wb + (u32)((t & 15) * (32 * F * 2) + (t >> 4) * 128), ldsb + slot * R3 + 8192 + lw * 2048); });
        }
        g64_load<1>(ldsb, lw, lane, ws + WS_HID + mt * 64 * 16, (const char*)A.w4 + nt * 128, (const char*)A.wo + nt * 128 - 28 * 65536, rot);
    } else {
        if (j < 28) {
            BAR();
            g3_compute(lds, w, lane, ws, WS_HID + (n3 * 16 * S + m3 * 128) * 16);
        } else BAR();
        const u32x4 e0 = ld16(ws, x1o), e1 = ld16(ws, x1o + S * 16);
        const float g = ld4(ws, WS_RST + m * 16 + 8);
        f32x16 y;
        const f32x16 a = mm64<36, GAP_G4>(lds, w, lane, y);
        PRIO(3);
#pragma unroll
        for (int k = 0; k < 8; ++k) { const f32x2 u = unp(k < 4 ? e0[k & 3] : e1[k & 3]); y[2 * k] = (y[2 * k] + u[0]) * (1.0f - g); y[2 * k + 1] = (y[2 * k + 1] + u[1]) * (1.0f - g); }
        u32 o[8];
#pragma unroll
        for (int k = 0; k < 8; ++k) o[k] = pk2(a[2 * k] * g + y[2 * k], a[2 * k + 1] * g + y[2 * k + 1]);
        *(u32x4*)(A.out + m * D + nc) = u32x4{o[0], o[1], o[2], o[3]}; *(u32x4*)(A.out + m * D + nc + 8) = u32x4{o[4], o[5], o[6], o[7]};
    }
}

extern "C" void kernel_launch(void* const* d_in, const int* in_sizes, int n_in, void* d_out, int out_size, void* d_ws, size_t ws_size,
                              hipStream_t stream) {
    Args A;
    A.x = (const u16*)d_in[0]; A.wqkv = (const u16*)d_in[1]; A.wo = (const u16*)d_in[2]; A.w1 = (const u16*)d_in[4]; A.w2 = (const u16*)d_in[5];
    A.w3 = (const u16*)d_in[6]; A.w4 = (const u16*)d_in[7]; A.gw = (const u16*)d_in[8]; A.ws = (char*)d_ws; A.out = (u16*)d_out;
    (void)hipFuncSetAttribute((const void*)mix_kernel, hipFuncAttributeMaxDynamicSharedMemorySize, LDS_BYTES);
    mix_kernel<<<512, 512, LDS_BYTES, stream>>>(A);
}
```
